```python
import torch

def _softmax(x, axis=-1):
    return torch.softmax(x, dim=axis)

def _gelu(x):
    return torch.nn.functional.gelu(x, approximate='tanh')

def _silu(x):
    return torch.nn.functional.silu(x)

def _sigmoid(x):
    return torch.sigmoid(x)

def _layernorm(x, axis=-1, eps=1e-05):
    m = x.mean(dim=axis, keepdim=True)
    v = x.var(dim=axis, keepdim=True, unbiased=False)
    return (x - m) / torch.sqrt(v + eps)

def _scatter_add(tgt, idx, src, axis=0):
    return tgt.clone().index_add_(axis, idx, src)

def _segment_sum(src, seg, n):
    out = torch.zeros((n,) + src.shape[1:], dtype=src.dtype)
    return out.index_add_(0, seg, src)

def _pad(x, pw):
    flat = tuple((v for pair in reversed(pw) for v in pair))
    return torch.nn.functional.pad(x, flat)
kernel_name = 'standalone__tensor_product_equiv_equiformer__graph'

def setup_inputs():

    _w = lambda a: (lambda t: t.to(torch.bfloat16) if t.is_floating_point() else t)(torch.as_tensor(a))
    N, deg, C, De = (10000, 32, 128, max(16, 1))
    E = N * deg
    src = (lambda lo, hi, *s: torch.randint(lo, hi, s))(0, N, E).to(torch.int64)
    dst = (lambda lo, hi, *s: torch.randint(lo, hi, s))(0, N, E).to(torch.int64)
    rvec = (lambda *s: torch.randn(*s, dtype=torch.bfloat16) * 0.05)(E, 3).to(float)
    rn = (torch.linalg.norm(rvec, dim=-1, keepdim=True) + 1e-06).to(float)
    rhat = (rvec / rn).to(float)
    eps = torch.zeros((3, 3, 3), dtype=float)
    eps[0, 1, 2] = eps[1, 2, 0] = eps[2, 0, 1] = 1.0
    eps[0, 2, 1] = eps[2, 1, 0] = eps[1, 0, 2] = -1.0
    return {'x': _w((lambda *s: torch.randn(*s, dtype=torch.bfloat16) * 0.2)(N, 4 * C).to(float)), 'rhat': _w(rhat), 'e_attr': _w((lambda *s: torch.randn(*s, dtype=torch.bfloat16) * 0.2)(E, De).to(float)), 'src': _w(src), 'dst': _w(dst), 'w_rad': _w((lambda *s: torch.randn(*s, dtype=torch.bfloat16) * 0.2)(De, 5 * C).to(float)), 'w_si0': _w((lambda *s: torch.randn(*s, dtype=torch.bfloat16) * 0.2)(2 * C, C).to(float)), 'w_si1': _w((lambda *s: torch.randn(*s, dtype=torch.bfloat16) * 0.05)(3 * C, C).to(float)), 'cg_eps': _w(eps)}

def reference(x, rhat, e_attr, src, dst, w_rad, w_si0, w_si1, cg_eps):
    N = x.shape[0]
    C = w_si0.shape[1]
    E = rhat.shape[0]
    x0 = x[:, :C].clone()
    x1 = x[:, C:].reshape(N, 3, C)
    R = (e_attr @ w_rad).reshape(E, 5, C)
    f0 = x0[src]
    f1 = x1[src]
    Y1 = rhat
    p00_0 = f0 * R[:, 0, :]
    p11_0 = torch.einsum('ejc,ej->ec', f1, Y1) * R[:, 1, :]
    p01_1 = torch.einsum('ec,ej->ejc', f0, Y1) * R[:, 2, :][:, None, :]
    p10_1 = f1 * R[:, 3, :][:, None, :]
    p11_1 = torch.einsum('ijk,ekc,ej->eic', cg_eps, f1, Y1) * R[:, 4, :][:, None, :]
    m0 = torch.concatenate([p00_0, p11_0], axis=-1) @ w_si0
    m1 = torch.einsum('ejp,pc->ejc', torch.concatenate([p01_1, p10_1, p11_1], axis=-1), w_si1)
    agg0 = _segment_sum(m0, dst, N)
    agg1 = _segment_sum(m1.reshape(E, 3 * C), dst, N).reshape(N, 3, C)
    gate = _sigmoid(agg0)
    out1 = agg1 * gate[:, None, :]
    return torch.concatenate([agg0, out1.reshape(N, 3 * C)], axis=-1)
```

```cpp
#include <hip/hip_runtime.h>
#include <hip/hip_bf16.h>
#include <stdint.h>


typedef unsigned short u16;
using bf16x8 = __attribute__((ext_vector_type(8))) short;
using f32x16 = __attribute__((ext_vector_type(16))) float;
using f32x2  = __attribute__((ext_vector_type(2))) float;
using f32x4  = __attribute__((ext_vector_type(4))) float;

#define CCH 128
#define CAP 64
#define AK 1408
#define ASTRIDE (AK + 8)
#define MAXNPB 20

__device__ __forceinline__ float bflo(uint32_t d) { return __uint_as_float(d << 16); }
__device__ __forceinline__ float bfhi(uint32_t d) { return __uint_as_float(d & 0xffff0000u); }
__device__ __forceinline__ float bf2f(u16 v) { return __uint_as_float(((uint32_t)v) << 16); }
__device__ __forceinline__ u16 f2bf(float f) {
  __hip_bfloat16 b = __float2bfloat16(f);
  return *reinterpret_cast<u16*>(&b);
}

__global__ __launch_bounds__(256) void prep_kernel(
    const int* __restrict__ src, const int* __restrict__ dst, int E,
    int* __restrict__ cnt, int2* __restrict__ bucket, int* __restrict__ ovf_cnt, int4* __restrict__ ovf,
    const u16* __restrict__ x, uint2* __restrict__ xr, int N,
    const u16* __restrict__ w0, const u16* __restrict__ w1, u16* __restrict__ w0t, u16* __restrict__ w1t,
    int nbE, int nbX) {
  int b = blockIdx.x;
  if (b < nbE) {
    int e = b * 256 + threadIdx.x;
    if (e < E) {
      int d = dst[e], s = src[e];
      int slot = atomicAdd(&cnt[d], 1);
      if (slot < CAP) {
        bucket[(size_t)d * CAP + slot] = make_int2(e, s);
      } else {
        int o = atomicAdd(ovf_cnt, 1);
        ovf[o] = make_int4(d, e, s, 0);
      }
    }
  } else if (b < nbE + nbX) {
    int i = (b - nbE) * 256 + threadIdx.x;
    if (i < N * CCH) {
      int n = i >> 7, ch = i & 127;
      const u16* row = x + (size_t)n * 512 + ch;
      uint32_t a0 = row[0], a1 = row[128], a2 = row[256], a3 = row[384];
      xr[(size_t)n * 128 + ch] = make_uint2(a0 | (a1 << 16), a2 | (a3 << 16));
    }
  } else {
    int i = (b - nbE - nbX) * 256 + threadIdx.x;
    if (i < 256 * 128) {
      int k = i >> 7, col = i & 127;
      w0t[col * 256 + k] = w0[i];
    } else if (i < 256 * 128 + 384 * 128) {
      int ii = i - 256 * 128;
      int k = ii >> 7, col = ii & 127;
      w1t[col * 384 + k] = w1[ii];
    }
  }
}

__global__ __launch_bounds__(256, 2) void main_kernel(
    const int* __restrict__ cnt, const int2* __restrict__ bucket, const int* __restrict__ ovf_cnt,
    const int4* __restrict__ ovf, const uint2* __restrict__ xr, const u16* __restrict__ rhat,
    const u16* __restrict__ e_attr, const u16* __restrict__ w_rad, const u16* __restrict__ cg,
    const u16* __restrict__ w0t, const u16* __restrict__ w1t, u16* __restrict__ out, int N, int npb) {
  __shared__ __attribute__((aligned(16))) u16 A_lds[MAXNPB][ASTRIDE];
  __shared__ __attribute__((aligned(16))) float ym[4][12][32];
  __shared__ int2 lscr[4][64];

  const int wave = threadIdx.x >> 6, lane = threadIdx.x & 63, h = lane >> 5, c = lane & 31;

  bf16x8 bw[5];
#pragma unroll
  for (int q = 0; q < 5; ++q)
#pragma unroll
    for (int j = 0; j < 8; ++j) bw[q][j] = (short)w_rad[(8 * h + j) * 640 + q * 128 + 32 * wave + c];
  float eps[27];
#pragma unroll
  for (int i = 0; i < 27; ++i) eps[i] = __uint_as_float(__builtin_amdgcn_readfirstlane(((int)cg[i]) << 16));

  const int n0 = blockIdx.x * npb;
  const int n1 = min(N, n0 + npb);
  const int novf = *ovf_cnt;

  for (int n = n0; n < n1; ++n) {
    int cn = min(cnt[n], CAP);
    int2 ent = make_int2(0x7fffffff, 0);
    if (lane < cn) ent = bucket[(size_t)n * CAP + lane];
    {
      int rank = 0;
      for (int j = 0; j < cn; ++j) {
        int kj = __builtin_amdgcn_readlane(ent.x, j);
        rank += (kj < ent.x) ? 1 : 0;
      }
      int ee = __builtin_amdgcn_ds_permute(rank << 2, ent.x);
      int ss = __builtin_amdgcn_ds_permute(rank << 2, ent.y);
      ent.x = ee; ent.y = ss;
    }
    f32x2 acc[11];
#pragma unroll
    for (int i = 0; i < 11; ++i) acc[i] = (f32x2){0.f, 0.f};

    int i0 = 0;
    while (true) {
      for (int base = 0; base < cn; base += 32) {
        const int m = min(32, cn - base);
        uint2 xv[16];
#pragma unroll
        for (int r = 0; r < 16; ++r) {
          int row = (r & 3) + 8 * (r >> 2) + 4 * h;
          int s = __shfl(ent.y, base + row);
          xv[r] = make_uint2(0u, 0u);
          if (row < m) xv[r] = xr[(size_t)s * 128 + wave * 32 + c];
        }
        int er = __shfl(ent.x, base + c);
        bool valid = c < m;
        bf16x8 a = (bf16x8){0, 0, 0, 0, 0, 0, 0, 0};
        if (valid) a = *reinterpret_cast<const bf16x8*>(e_attr + (size_t)er * 16 + 8 * h);
        if (h == 0) {
          float y0 = 0.f, y1 = 0.f, y2 = 0.f;
          if (valid) {
            y0 = bf2f(rhat[(size_t)er * 3 + 0]);
            y1 = bf2f(rhat[(size_t)er * 3 + 1]);
            y2 = bf2f(rhat[(size_t)er * 3 + 2]);
          }
          ym[wave][0][c] = y0; ym[wave][1][c] = y1; ym[wave][2][c] = y2;
#pragma unroll
          for (int i = 0; i < 3; ++i)
#pragma unroll
            for (int k = 0; k < 3; ++k)
              ym[wave][3 + i * 3 + k][c] =
                  eps[i * 9 + 0 * 3 + k] * y0 + eps[i * 9 + 1 * 3 + k] * y1 + eps[i * 9 + 2 * 3 + k] * y2;
        }
        f32x16 R[5];
        const f32x16 z16 = {0.f, 0.f, 0.f, 0.f, 0.f, 0.f, 0.f, 0.f, 0.f, 0.f, 0.f, 0.f, 0.f, 0.f, 0.f, 0.f};
#pragma unroll
        for (int q = 0; q < 5; ++q) R[q] = __builtin_amdgcn_mfma_f32_32x32x16_bf16(a, bw[q], z16, 0, 0, 0);
        __builtin_amdgcn_s_waitcnt(0xc07f);
        __builtin_amdgcn_wave_barrier();
#pragma unroll
        for (int g = 0; g < 4; ++g) {
          if (8 * g < m) {
#pragma unroll
            for (int p = 0; p < 2; ++p) {
              const int r0 = 4 * g + 2 * p;
              f32x2 f0 = {bflo(xv[r0].x), bflo(xv[r0 + 1].x)};
              f32x2 fx = {bfhi(xv[r0].x), bfhi(xv[r0 + 1].x)};
              f32x2 fy = {bflo(xv[r0].y), bflo(xv[r0 + 1].y)};
              f32x2 fz = {bfhi(xv[r0].y), bfhi(xv[r0 + 1].y)};
              f32x2 R0 = {R[0][r0], R[0][r0 + 1]};
              f32x2 R1 = {R[1][r0], R[1][r0 + 1]};
              f32x2 R2 = {R[2][r0], R[2][r0 + 1]};
              f32x2 R3 = {R[3][r0], R[3][r0 + 1]};
              f32x2 R4 = {R[4][r0], R[4][r0 + 1]};
              f32x2 Y[12];
#pragma unroll
              for (int i = 0; i < 12; ++i) Y[i] = *reinterpret_cast<const f32x2*>(&ym[wave][i][8 * g + 4 * h + 2 * p]);
              acc[0] += f0 * R0;
              f32x2 d = fx * Y[0] + fy * Y[1] + fz * Y[2];
              acc[1] += d * R1;
              f32x2 t = f0 * R2;
              acc[2] += t * Y[0];
              acc[5] += t * Y[1];
              acc[8] += t * Y[2];
              acc[3] += fx * R3;
              acc[6] += fy * R3;
              acc[9] += fz * R3;
              f32x2 cx = fx * Y[3] + fy * Y[4] + fz * Y[5];
              f32x2 cy = fx * Y[6] + fy * Y[7] + fz * Y[8];
              f32x2 cz = fx * Y[9] + fy * Y[10] + fz * Y[11];
              acc[4] += cx * R4;
              acc[7] += cy * R4;
              acc[10] += cz * R4;
            }
          }
        }
        __builtin_amdgcn_s_waitcnt(0xc07f);
        __builtin_amdgcn_wave_barrier();
      }
      if (novf == 0) break;
      bool found = false;
      while (i0 < novf) {
        int4 o = make_int4(-1, 0, 0, 0);
        if (i0 + lane < novf) o = ovf[i0 + lane];
        i0 += 64;
        bool match = (o.x == n);
        unsigned long long mask = __ballot(match);
        if (mask) {
          int pos = __popcll(mask & ((1ull << lane) - 1ull));
          if (match) lscr[wave][pos] = make_int2(o.y, o.z);
          __builtin_amdgcn_s_waitcnt(0xc07f);
          __builtin_amdgcn_wave_barrier();
          cn = __popcll(mask);
          ent = make_int2(0, 0);
          if (lane < cn) ent = lscr[wave][lane];
          __builtin_amdgcn_s_waitcnt(0xc07f);
          __builtin_amdgcn_wave_barrier();
          found = true;
          break;
        }
      }
      if (!found) break;
    }
    const int slot = n - n0;
#pragma unroll
    for (int i = 0; i < 11; ++i) {
      float s = acc[i][0] + acc[i][1];
      s += __shfl_xor(s, 32);
      if (h == 0) A_lds[slot][i * 128 + 32 * wave + c] = f2bf(s);
    }
  }
  __syncthreads();

  const int nv = n1 - n0;
  const int arow = min(c, MAXNPB - 1);
  const f32x16 z16 = {0.f, 0.f, 0.f, 0.f, 0.f, 0.f, 0.f, 0.f, 0.f, 0.f, 0.f, 0.f, 0.f, 0.f, 0.f, 0.f};
  f32x16 o0 = z16;
  {
    const u16* wrow = w0t + (size_t)(32 * wave + c) * 256 + 8 * h;
#pragma unroll 4
    for (int s = 0; s < 16; ++s) {
      bf16x8 a = *reinterpret_cast<const bf16x8*>(&A_lds[arow][16 * s + 8 * h]);
      bf16x8 b = *reinterpret_cast<const bf16x8*>(wrow + 16 * s);
      o0 = __builtin_amdgcn_mfma_f32_32x32x16_bf16(a, b, o0, 0, 0, 0);
    }
  }
  float gate[16];
#pragma unroll
  for (int r = 0; r < 16; ++r) {
    int row = (r & 3) + 8 * (r >> 2) + 4 * h;
    gate[r] = 1.f / (1.f + __expf(-o0[r]));
    if (row < nv) out[(size_t)(n0 + row) * 512 + 32 * wave + c] = f2bf(o0[r]);
  }
  const u16* wrow1 = w1t + (size_t)(32 * wave + c) * 384 + 8 * h;
  for (int j = 0; j < 3; ++j) {
    f32x16 o1 = z16;
#pragma unroll 4
    for (int s = 0; s < 24; ++s) {
      bf16x8 a = *reinterpret_cast<const bf16x8*>(&A_lds[arow][256 + j * 384 + 16 * s + 8 * h]);
      bf16x8 b = *reinterpret_cast<const bf16x8*>(wrow1 + 16 * s);
      o1 = __builtin_amdgcn_mfma_f32_32x32x16_bf16(a, b, o1, 0, 0, 0);
    }
#pragma unroll
    for (int r = 0; r < 16; ++r) {
      int row = (r & 3) + 8 * (r >> 2) + 4 * h;
      if (row < nv) out[(size_t)(n0 + row) * 512 + 128 + j * 128 + 32 * wave + c] = f2bf(o1[r] * gate[r]);
    }
  }
}

extern "C" void kernel_launch(void* const* d_in, const int* in_sizes, int n_in,
                              void* d_out, int out_size, void* d_ws, size_t ws_size,
                              hipStream_t stream) {
  const u16* x = (const u16*)d_in[0];
  const u16* rhat = (const u16*)d_in[1];
  const u16* e_attr = (const u16*)d_in[2];
  const int* src = (const int*)d_in[3];
  const int* dst = (const int*)d_in[4];
  const u16* w_rad = (const u16*)d_in[5];
  const u16* w_si0 = (const u16*)d_in[6];
  const u16* w_si1 = (const u16*)d_in[7];
  const u16* cg = (const u16*)d_in[8];
  u16* out = (u16*)d_out;
  const int N = in_sizes[0] / 512;
  const int E = in_sizes[3];

  char* ws = (char*)d_ws;
  size_t off = 0;
  auto take = [&](size_t bytes) { size_t o = off; off += (bytes + 255) & ~(size_t)255; return o; };
  size_t o_cnt = take((size_t)(N + 1) * 4);
  size_t o_bucket = take((size_t)N * CAP * 8);
  size_t o_ovf = take((size_t)E * 16);
  size_t o_xr = take((size_t)N * 1024);
  size_t o_w0t = take(256 * 128 * 2);
  size_t o_w1t = take(384 * 128 * 2);
  int* cnt = (int*)(ws + o_cnt);
  int* ovf_cnt = cnt + N;
  int2* bucket = (int2*)(ws + o_bucket);
  int4* ovf = (int4*)(ws + o_ovf);
  uint2* xr = (uint2*)(ws + o_xr);
  u16* w0t = (u16*)(ws + o_w0t);
  u16* w1t = (u16*)(ws + o_w1t);

  hipMemsetAsync(cnt, 0, (size_t)(N + 1) * 4, stream);
  int nbE = (E + 255) / 256;
  int nbX = (N * CCH + 255) / 256;
  int nbW = (640 * 128 + 255) / 256;
  prep_kernel<<<nbE + nbX + nbW, 256, 0, stream>>>(src, dst, E, cnt, bucket, ovf_cnt, ovf, x, xr, N,
                                                   w_si0, w_si1, w0t, w1t, nbE, nbX);
  int G = 512;
  int npb = (N + G - 1) / G;
  if (npb > MAXNPB) npb = MAXNPB;
  if (npb < 1) npb = 1;
  G = (N + npb - 1) / npb;
  main_kernel<<<G, 256, 0, stream>>>(cnt, bucket, ovf_cnt, ovf, xr, rhat, e_attr, w_rad, cg, w0t, w1t, out, N, npb);
}
```

```cpp
#include <hip/hip_runtime.h>
#include <hip/hip_bf16.h>
#include <stdint.h>


typedef unsigned short u16;
using bf16x8 = __attribute__((ext_vector_type(8))) short;
using f32x16 = __attribute__((ext_vector_type(16))) float;
using f32x2  = __attribute__((ext_vector_type(2))) float;
using f32x4  = __attribute__((ext_vector_type(4))) float;

#define CCH 128
#define CH 2048
#define LCAP 1024
#define AK 1408
#define ASTRIDE (AK + 8)
#define MAXNPB 20

__device__ __forceinline__ float bflo(uint32_t d) { return __uint_as_float(d << 16); }
__device__ __forceinline__ float bfhi(uint32_t d) { return __uint_as_float(d & 0xffff0000u); }
__device__ __forceinline__ float bf2f(u16 v) { return __uint_as_float(((uint32_t)v) << 16); }
__device__ __forceinline__ u16 f2bf(float f) {
  __hip_bfloat16 b = __float2bfloat16(f);
  return *reinterpret_cast<u16*>(&b);
}


struct TileRegs { uint2 xv[16]; bf16x8 a; float y0, y1, y2; };

__global__ __launch_bounds__(256) void prep_kernel(
    const int* __restrict__ src, const int* __restrict__ dst, int E, int npb, int NB,
    int* __restrict__ offs, int4* __restrict__ sorted,
    const u16* __restrict__ x, uint4* __restrict__ xr, int N,
    const u16* __restrict__ w0, const u16* __restrict__ w1, u16* __restrict__ w0t, u16* __restrict__ w1t,
    int nbE, int nbX) {
  extern __shared__ int hist[];
  int b = blockIdx.x;
  const int tid = threadIdx.x;
  if (b < nbE) {
    int* part = hist + NB + 1;
    for (int i = tid; i <= NB; i += 256) hist[i] = 0;
    __syncthreads();
    int bin[CH / 256], rk[CH / 256];
#pragma unroll
    for (int i = 0; i < CH / 256; ++i) {
      int e = b * CH + i * 256 + tid;
      bin[i] = -1; rk[i] = 0;
      if (e < E) { bin[i] = dst[e] / npb; rk[i] = atomicAdd(&hist[bin[i]], 1); }
    }
    __syncthreads();
    const int per = (NB + 255) / 256;
    int lsum = 0;
    for (int j = 0; j < per; ++j) { int k = tid * per + j; if (k < NB) lsum += hist[k]; }
    part[tid] = lsum;
    __syncthreads();
    for (int o = 1; o < 256; o <<= 1) {
      int v = (tid >= o) ? part[tid - o] : 0;
      __syncthreads();
      part[tid] += v;
      __syncthreads();
    }
    int run = part[tid] - lsum;
    for (int j = 0; j < per; ++j) {
      int k = tid * per + j;
      if (k < NB) { int v = hist[k]; hist[k] = run; run += v; }
    }
    if (tid == 255) hist[NB] = part[255];
    __syncthreads();
    for (int i = tid; i <= NB; i += 256) offs[(size_t)b * (NB + 1) + i] = hist[i];
#pragma unroll
    for (int i = 0; i < CH / 256; ++i) {
      int e = b * CH + i * 256 + tid;
      if (e < E) sorted[(size_t)b * CH + hist[bin[i]] + rk[i]] = make_int4(e, src[e], dst[e], 0);
    }
  } else if (b < nbE + nbX) {
    int i = (b - nbE) * 256 + tid;
    if (i < N * 16) {
      int n = i >> 4, cg8 = (i & 15) * 8;
      const u16* row = x + (size_t)n * 512 + cg8;
      uint4 q0 = *reinterpret_cast<const uint4*>(row);
      uint4 q1 = *reinterpret_cast<const uint4*>(row + 128);
      uint4 q2 = *reinterpret_cast<const uint4*>(row + 256);
      uint4 q3 = *reinterpret_cast<const uint4*>(row + 384);
      const uint32_t* p0 = reinterpret_cast<const uint32_t*>(&q0);
      const uint32_t* p1 = reinterpret_cast<const uint32_t*>(&q1);
      const uint32_t* p2 = reinterpret_cast<const uint32_t*>(&q2);
      const uint32_t* p3 = reinterpret_cast<const uint32_t*>(&q3);
      uint4* o = xr + ((size_t)n * 128 + cg8) / 2;
#pragma unroll
      for (int k = 0; k < 4; ++k) {
        uint4 v;
        v.x = (p0[k] & 0xffffu) | (p1[k] << 16);
        v.y = (p2[k] & 0xffffu) | (p3[k] << 16);
        v.z = (p0[k] >> 16) | (p1[k] & 0xffff0000u);
        v.w = (p2[k] >> 16) | (p3[k] & 0xffff0000u);
        o[k] = v;
      }
    }
  } else {
    int i = (b - nbE - nbX) * 256 + tid;
    if (i < 256 * 128) {
      int k = i >> 7, col = i & 127;
      w0t[col * 256 + k] = w0[i];
    } else if (i < 256 * 128 + 384 * 128) {
      int ii = i - 256 * 128;
      int k = ii >> 7, col = ii & 127;
      w1t[col * 384 + k] = w1[ii];
    }
  }
}

#define WAVE_SYNC() do { __builtin_amdgcn_s_waitcnt(0xc07f); __builtin_amdgcn_wave_barrier(); } while (0)

__device__ __forceinline__ void load_tile(TileRegs& t, int le, int ls, int m, int h, int c, int wave,
                                          const uint2* __restrict__ xr, const u16* __restrict__ e_attr,
                                          const u16* __restrict__ rhat) {
#pragma unroll
  for (int r = 0; r < 16; ++r) {
    int row = (r & 3) + 8 * (r >> 2) + 4 * h;
    int s = __shfl(ls, row);
    t.xv[r] = make_uint2(0u, 0u);
    if (row < m) t.xv[r] = xr[(size_t)s * 128 + wave * 32 + c];
  }
  t.a = (bf16x8){0, 0, 0, 0, 0, 0, 0, 0};
  t.y0 = 0.f; t.y1 = 0.f; t.y2 = 0.f;
  if (c < m) {
    t.a = *reinterpret_cast<const bf16x8*>(e_attr + (size_t)le * 16 + 8 * h);
    t.y0 = bf2f(rhat[(size_t)le * 3 + 0]);
    t.y1 = bf2f(rhat[(size_t)le * 3 + 1]);
    t.y2 = bf2f(rhat[(size_t)le * 3 + 2]);
  }
}

__device__ __forceinline__ void compute_tile(const TileRegs& t, int m, int h, int c, float (*ymw)[32],
                                             const float* eps, const bf16x8* bw, f32x2* acc) {
  if (h == 0) {
    ymw[0][c] = t.y0; ymw[1][c] = t.y1; ymw[2][c] = t.y2;
#pragma unroll
    for (int i = 0; i < 3; ++i)
#pragma unroll
      for (int k = 0; k < 3; ++k)
        ymw[3 + i * 3 + k][c] = eps[i * 9 + k] * t.y0 + eps[i * 9 + 3 + k] * t.y1 + eps[i * 9 + 6 + k] * t.y2;
  }
  const f32x16 z16 = {0.f, 0.f, 0.f, 0.f, 0.f, 0.f, 0.f, 0.f, 0.f, 0.f, 0.f, 0.f, 0.f, 0.f, 0.f, 0.f};
  {
    f32x16 Ra = __builtin_amdgcn_mfma_f32_32x32x16_bf16(t.a, bw[0], z16, 0, 0, 0);
    f32x16 Rb = __builtin_amdgcn_mfma_f32_32x32x16_bf16(t.a, bw[1], z16, 0, 0, 0);
    f32x16 Rc = __builtin_amdgcn_mfma_f32_32x32x16_bf16(t.a, bw[2], z16, 0, 0, 0);
    WAVE_SYNC();
#pragma unroll
    for (int g = 0; g < 4; ++g) {
      if (8 * g < m) {
#pragma unroll
        for (int p = 0; p < 2; ++p) {
          const int r0 = 4 * g + 2 * p;
          f32x2 f0 = {bflo(t.xv[r0].x), bflo(t.xv[r0 + 1].x)};
          f32x2 fx = {bfhi(t.xv[r0].x), bfhi(t.xv[r0 + 1].x)};
          f32x2 fy = {bflo(t.xv[r0].y), bflo(t.xv[r0 + 1].y)};
          f32x2 fz = {bfhi(t.xv[r0].y), bfhi(t.xv[r0 + 1].y)};
          f32x2 R0 = {Ra[r0], Ra[r0 + 1]};
          f32x2 R1 = {Rb[r0], Rb[r0 + 1]};
          f32x2 R2 = {Rc[r0], Rc[r0 + 1]};
          f32x2 Y[3];
#pragma unroll
          for (int i = 0; i < 3; ++i) Y[i] = *reinterpret_cast<const f32x2*>(&ymw[i][8 * g + 4 * h + 2 * p]);
          acc[0] += f0 * R0;
          f32x2 d = fx * Y[0] + fy * Y[1] + fz * Y[2];
          acc[1] += d * R1;
          f32x2 tt = f0 * R2;
          acc[2] += tt * Y[0];
          acc[5] += tt * Y[1];
          acc[8] += tt * Y[2];
        }
      }
    }
  }
  {
    f32x16 Rd = __builtin_amdgcn_mfma_f32_32x32x16_bf16(t.a, bw[3], z16, 0, 0, 0);
    f32x16 Re = __builtin_amdgcn_mfma_f32_32x32x16_bf16(t.a, bw[4], z16, 0, 0, 0);
#pragma unroll
    for (int g = 0; g < 4; ++g) {
      if (8 * g < m) {
#pragma unroll
        for (int p = 0; p < 2; ++p) {
          const int r0 = 4 * g + 2 * p;
          f32x2 fx = {bfhi(t.xv[r0].x), bfhi(t.xv[r0 + 1].x)};
          f32x2 fy = {bflo(t.xv[r0].y), bflo(t.xv[r0 + 1].y)};
          f32x2 fz = {bfhi(t.xv[r0].y), bfhi(t.xv[r0 + 1].y)};
          f32x2 R3 = {Rd[r0], Rd[r0 + 1]};
          f32x2 R4 = {Re[r0], Re[r0 + 1]};
          f32x2 Y[12];
#pragma unroll
          for (int i = 3; i < 12; ++i) Y[i] = *reinterpret_cast<const f32x2*>(&ymw[i][8 * g + 4 * h + 2 * p]);
          acc[3] += fx * R3;
          acc[6] += fy * R3;
          acc[9] += fz * R3;
          f32x2 cx = fx * Y[3] + fy * Y[4] + fz * Y[5];
          f32x2 cy = fx * Y[6] + fy * Y[7] + fz * Y[8];
          f32x2 cz = fx * Y[9] + fy * Y[10] + fz * Y[11];
          acc[4] += cx * R4;
          acc[7] += cy * R4;
          acc[10] += cz * R4;
        }
      }
    }
  }
  WAVE_SYNC();
}

__global__ __launch_bounds__(256, 2) void main_kernel(
    const int* __restrict__ offs, const int4* __restrict__ sorted, int nblk, int NB,
    const uint2* __restrict__ xr, const u16* __restrict__ rhat,
    const u16* __restrict__ e_attr, const u16* __restrict__ w_rad, const u16* __restrict__ cg,
    const u16* __restrict__ w0t, const u16* __restrict__ w1t, u16* __restrict__ out, int N, int npb) {
  __shared__ __attribute__((aligned(16))) u16 A_lds[MAXNPB][ASTRIDE];
  __shared__ __attribute__((aligned(16))) float ym[4][12][32];
  __shared__ int2 fin[LCAP];
  __shared__ int segoff[260];
  __shared__ int seglo[256];
  __shared__ int nh[MAXNPB + 1];
  __shared__ int noff[MAXNPB + 2];
  __shared__ int2 lscr[4][64];
  int2* tmp = reinterpret_cast<int2*>(&A_lds[0][0]);

  const int tid = threadIdx.x;
  const int wave = tid >> 6, lane = tid & 63, h = lane >> 5, c = lane & 31;
  const int bin = blockIdx.x;
  const int n0 = bin * npb;
  const int n1 = min(N, n0 + npb);
  const int nv = n1 - n0;

  bf16x8 bw[5];
#pragma unroll
  for (int q = 0; q < 5; ++q)
#pragma unroll
    for (int j = 0; j < 8; ++j) bw[q][j] = (short)w_rad[(8 * h + j) * 640 + q * 128 + 32 * wave + c];
  float eps[27];
#pragma unroll
  for (int i = 0; i < 27; ++i) eps[i] = __uint_as_float(__builtin_amdgcn_readfirstlane(((int)cg[i]) << 16));

  bool fast = (nblk <= 256);
  if (tid <= MAXNPB) nh[tid] = 0;
  if (fast) {
    int cntb = 0;
    if (tid < nblk) {
      int lo = offs[(size_t)tid * (NB + 1) + bin], hi = offs[(size_t)tid * (NB + 1) + bin + 1];
      seglo[tid] = lo; cntb = hi - lo;
    }
    segoff[tid] = cntb;
    __syncthreads();
    for (int o = 1; o < 256; o <<= 1) {
      int v = (tid >= o) ? segoff[tid - o] : 0;
      __syncthreads();
      segoff[tid] += v;
      __syncthreads();
    }
    fast = (segoff[255] <= LCAP);
  }
  const int total = fast ? segoff[255] : 0;
  if (fast) {
    int myslot[LCAP / 256], myr[LCAP / 256];
    int2 mine[LCAP / 256];
#pragma unroll
    for (int k = 0; k < LCAP / 256; ++k) {
      int i = tid + 256 * k;
      myslot[k] = -1;
      if (i < total) {
        int lo = 0, hi = nblk - 1;
        while (lo < hi) { int mid = (lo + hi) >> 1; if (segoff[mid] > i) hi = mid; else lo = mid + 1; }
        int start = (lo > 0) ? segoff[lo - 1] : 0;
        int4 v = sorted[(size_t)lo * CH + seglo[lo] + (i - start)];
        mine[k] = make_int2(v.x, v.y);
        myslot[k] = v.z - n0;
        myr[k] = atomicAdd(&nh[myslot[k]], 1);
      }
    }
    __syncthreads();
    if (tid == 0) { int run = 0; for (int i = 0; i < nv; ++i) { noff[i] = run; run += nh[i]; } noff[nv] = run; }
    __syncthreads();
#pragma unroll
    for (int k = 0; k < LCAP / 256; ++k)
      if (myslot[k] >= 0) tmp[noff[myslot[k]] + myr[k]] = mine[k];
    __syncthreads();
#pragma unroll
    for (int k = 0; k < LCAP / 256; ++k)
      if (myslot[k] >= 0) {
        int a0 = noff[myslot[k]], a1 = noff[myslot[k] + 1], rank = 0;
        for (int j = a0; j < a1; ++j) rank += (tmp[j].x < mine[k].x) ? 1 : 0;
        fin[a0 + rank] = mine[k];
      }
  }
  __syncthreads();

  f32x2 acc[11];
#pragma unroll
  for (int i = 0; i < 11; ++i) acc[i] = (f32x2){0.f, 0.f};
  float (*ymw)[32] = ym[wave];

  if (fast) {
    TileRegs cur, nxt;
    int s = 0, base = 0;
    {
      int a0 = noff[0], cn = noff[1] - a0;
      int m = min(32, cn);
      int2 en = make_int2(0, 0);
      if (c < m) en = fin[a0 + c];
      load_tile(cur, en.x, en.y, m, h, c, wave, xr, e_attr, rhat);
    }
    while (s < nv) {
      const int cn = noff[s + 1] - noff[s];
      const int m = min(32, cn - base);
      int ns = s, nb = base + 32;
      if (nb >= cn) { ns = s + 1; nb = 0; }
      if (ns < nv) {
        int a0 = noff[ns], cn2 = noff[ns + 1] - a0;
        int m2 = min(32, cn2 - nb);
        int2 en = make_int2(0, 0);
        if (c < m2) en = fin[a0 + nb + c];
        load_tile(nxt, en.x, en.y, m2, h, c, wave, xr, e_attr, rhat);
      }
      if (m > 0) compute_tile(cur, m, h, c, ymw, eps, bw, acc);
      if (nb == 0) {
#pragma unroll
        for (int i = 0; i < 11; ++i) {
          float v = acc[i][0] + acc[i][1];
          v += __shfl_xor(v, 32);
          if (h == 0) A_lds[s][i * 128 + 32 * wave + c] = f2bf(v);
          acc[i] = (f32x2){0.f, 0.f};
        }
      }
      cur = nxt; s = ns; base = nb;
    }
  } else {
    for (int s = 0; s < nv; ++s) {
      const int n = n0 + s;
      for (int t = 0; t < nblk; ++t) {
        int lo = offs[(size_t)t * (NB + 1) + bin], hi = offs[(size_t)t * (NB + 1) + bin + 1];
        for (int i0 = lo; i0 < hi; i0 += 64) {
          int4 o = make_int4(0, 0, -1, 0);
          if (i0 + lane < hi) o = sorted[(size_t)t * CH + i0 + lane];
          bool match = (o.z == n);
          unsigned long long mask = __ballot(match);
          if (mask) {
            int pos = __popcll(mask & ((1ull << lane) - 1ull));
            if (match) lscr[wave][pos] = make_int2(o.x, o.y);
            WAVE_SYNC();
            int cn = __popcll(mask);
            for (int base = 0; base < cn; base += 32) {
              int m = min(32, cn - base);
              int2 en = make_int2(0, 0);
              if (c < m) en = lscr[wave][base + c];
              TileRegs cur;
              load_tile(cur, en.x, en.y, m, h, c, wave, xr, e_attr, rhat);
              compute_tile(cur, m, h, c, ymw, eps, bw, acc);
            }
            WAVE_SYNC();
          }
        }
      }
#pragma unroll
      for (int i = 0; i < 11; ++i) {
        float v = acc[i][0] + acc[i][1];
        v += __shfl_xor(v, 32);
        if (h == 0) A_lds[s][i * 128 + 32 * wave + c] = f2bf(v);
        acc[i] = (f32x2){0.f, 0.f};
      }
    }
  }
  __syncthreads();

  const int arow = min(c, MAXNPB - 1);
  const f32x16 z16 = {0.f, 0.f, 0.f, 0.f, 0.f, 0.f, 0.f, 0.f, 0.f, 0.f, 0.f, 0.f, 0.f, 0.f, 0.f, 0.f};
  f32x16 o0 = z16;
  {
    const u16* wrow = w0t + (size_t)(32 * wave + c) * 256 + 8 * h;
#pragma unroll 4
    for (int s = 0; s < 16; ++s) {
      bf16x8 a = *reinterpret_cast<const bf16x8*>(&A_lds[arow][16 * s + 8 * h]);
      bf16x8 b = *reinterpret_cast<const bf16x8*>(wrow + 16 * s);
      o0 = __builtin_amdgcn_mfma_f32_32x32x16_bf16(a, b, o0, 0, 0, 0);
    }
  }
  float gate[16];
#pragma unroll
  for (int r = 0; r < 16; ++r) {
    int row = (r & 3) + 8 * (r >> 2) + 4 * h;
    gate[r] = 1.f / (1.f + __expf(-o0[r]));
    if (row < nv) out[(size_t)(n0 + row) * 512 + 32 * wave + c] = f2bf(o0[r]);
  }
  const u16* wrow1 = w1t + (size_t)(32 * wave + c) * 384 + 8 * h;
  for (int j = 0; j < 3; ++j) {
    f32x16 o1 = z16;
#pragma unroll 4
    for (int s = 0; s < 24; ++s) {
      bf16x8 a = *reinterpret_cast<const bf16x8*>(&A_lds[arow][256 + j * 384 + 16 * s + 8 * h]);
      bf16x8 b = *reinterpret_cast<const bf16x8*>(wrow1 + 16 * s);
      o1 = __builtin_amdgcn_mfma_f32_32x32x16_bf16(a, b, o1, 0, 0, 0);
    }
#pragma unroll
    for (int r = 0; r < 16; ++r) {
      int row = (r & 3) + 8 * (r >> 2) + 4 * h;
      if (row < nv) out[(size_t)(n0 + row) * 512 + 128 + j * 128 + 32 * wave + c] = f2bf(o1[r] * gate[r]);
    }
  }
}

extern "C" void kernel_launch(void* const* d_in, const int* in_sizes, int n_in,
                              void* d_out, int out_size, void* d_ws, size_t ws_size,
                              hipStream_t stream) {
  const u16* x = (const u16*)d_in[0];
  const u16* rhat = (const u16*)d_in[1];
  const u16* e_attr = (const u16*)d_in[2];
  const int* src = (const int*)d_in[3];
  const int* dst = (const int*)d_in[4];
  const u16* w_rad = (const u16*)d_in[5];
  const u16* w_si0 = (const u16*)d_in[6];
  const u16* w_si1 = (const u16*)d_in[7];
  const u16* cg = (const u16*)d_in[8];
  u16* out = (u16*)d_out;
  const int N = in_sizes[0] / 512;
  const int E = in_sizes[3];

  int G = 512;
  int npb = (N + G - 1) / G;
  if (npb > MAXNPB) npb = MAXNPB;
  if (npb < 1) npb = 1;
  G = (N + npb - 1) / npb;
  const int NB = G;
  const int nbE = (E + CH - 1) / CH;

  char* ws = (char*)d_ws;
  size_t off = 0;
  auto take = [&](size_t bytes) { size_t o = off; off += (bytes + 255) & ~(size_t)255; return o; };
  size_t o_offs = take((size_t)nbE * (NB + 1) * 4);
  size_t o_sorted = take((size_t)nbE * CH * 16);
  size_t o_xr = take((size_t)N * 1024);
  size_t o_w0t = take(256 * 128 * 2);
  size_t o_w1t = take(384 * 128 * 2);
  int* offs = (int*)(ws + o_offs);
  int4* sorted = (int4*)(ws + o_sorted);
  uint4* xr = (uint4*)(ws + o_xr);
  u16* w0t = (u16*)(ws + o_w0t);
  u16* w1t = (u16*)(ws + o_w1t);

  int nbX = (N * 16 + 255) / 256;
  int nbW = (640 * 128 + 255) / 256;
  size_t lds = (size_t)(NB + 1 + 256) * 4;
  prep_kernel<<<nbE + nbX + nbW, 256, lds, stream>>>(src, dst, E, npb, NB, offs, sorted, x, xr, N,
                                                     w_si0, w_si1, w0t, w1t, nbE, nbX);
  main_kernel<<<G, 256, 0, stream>>>(offs, sorted, nbE, NB, (const uint2*)xr, rhat, e_attr, w_rad, cg, w0t, w1t,
                                     out, N, npb);
}
```
